```python
import torch

def _softmax(x, axis=-1):
    return torch.softmax(x, dim=axis)
def _gelu(x):
    return torch.nn.functional.gelu(x, approximate="tanh")
def _silu(x):
    return torch.nn.functional.silu(x)
def _sigmoid(x):
    return torch.sigmoid(x)
def _layernorm(x, axis=-1, eps=1e-5):
    m = x.mean(dim=axis, keepdim=True)
    v = x.var(dim=axis, keepdim=True, unbiased=False)
    return (x - m) / torch.sqrt(v + eps)
def _scatter_add(tgt, idx, src, axis=0):
    return tgt.clone().index_add_(axis, idx, src)
def _segment_sum(src, seg, n):
    out = torch.zeros((n,) + src.shape[1:], dtype=src.dtype)
    return out.index_add_(0, seg, src)
def _pad(x, pw):
    flat = tuple(v for pair in reversed(pw) for v in pair)
    return torch.nn.functional.pad(x, flat)

kernel_name = 'standalone__channel_attn_se__vision'

def setup_inputs():

    _w = (lambda a: (lambda t: t.to(torch.bfloat16) if t.is_floating_point() else t)(torch.as_tensor(a)))
    H = W = 512
    C = 192
    r = max(C // 16, 4)
    return {
        "x": _w((lambda *s: torch.randn(*s, dtype=torch.bfloat16) * 3.2)(1, H, W, C)),
        "w_down": _w((lambda *s: torch.randn(*s, dtype=torch.bfloat16) * 3.2)(C, r)),
        "w_up": _w((lambda *s: torch.randn(*s, dtype=torch.bfloat16) * 3.2)(r, C)),
    }

def reference(x, w_down, w_up):
    s = x.mean(axis=(1, 2))
    h = _silu(torch.einsum('bc,cr->br', s, w_down))
    g = _sigmoid(torch.einsum('br,rc->bc', h, w_up))
    return x * g[:, None, None, :]
```

```cpp
#include <hip/hip_runtime.h>
#include <hip/hip_bf16.h>
#include <stdint.h>

typedef unsigned v4u __attribute__((ext_vector_type(4)));

constexpr int C = 192;
constexpr int R = 12;
constexpr int NB1 = 512;
constexpr int T1 = 768;

__device__ __forceinline__ float bf_lo(unsigned u) { return __uint_as_float(u << 16); }
__device__ __forceinline__ float bf_hi(unsigned u) { return __uint_as_float(u & 0xffff0000u); }
__device__ __forceinline__ float rbf(float f) { return __bfloat162float(__float2bfloat16(f)); }

__global__ __launch_bounds__(T1) void k_partial(const v4u* __restrict__ x, float* __restrict__ part, int nchunks) {
    __shared__ float red[T1 * 8];
    int per = nchunks / NB1;
    const v4u* p = x + (size_t)blockIdx.x * per + threadIdx.x;
    float acc[8] = {0, 0, 0, 0, 0, 0, 0, 0};
    constexpr int U = 16;
    for (int i = 0; i < per; i += T1 * U) {
        v4u v[U];
#pragma unroll
        for (int u = 0; u < U; ++u) v[u] = __builtin_nontemporal_load(p + i + u * T1);
#pragma unroll
        for (int u = 0; u < U; ++u) {
#pragma unroll
            for (int j = 0; j < 4; ++j) { acc[2 * j] += bf_lo(v[u][j]); acc[2 * j + 1] += bf_hi(v[u][j]); }
        }
    }
#pragma unroll
    for (int j = 0; j < 8; ++j) red[threadIdx.x * 8 + j] = acc[j];
    __syncthreads();
    if (threadIdx.x < C) {
        float s = 0;
        for (int k = 0; k < T1 / 24; ++k) s += red[k * C + threadIdx.x];
        part[blockIdx.x * C + threadIdx.x] = s;
    }
}

__global__ __launch_bounds__(768) void k_gate(const float* __restrict__ part, const __hip_bfloat16* __restrict__ wd,
                                              const __hip_bfloat16* __restrict__ wu, float* __restrict__ g, float inv_n) {
    __shared__ float red[4 * C];
    __shared__ float s[C];
    __shared__ float h[R];
    int c = threadIdx.x % C, q = threadIdx.x / C;
    float a = 0;
#pragma unroll 16
    for (int b = q; b < NB1; b += 4) a += part[b * C + c];
    red[q * C + c] = a;
    __syncthreads();
    if (threadIdx.x < C) s[c] = rbf((red[c] + red[C + c] + red[2 * C + c] + red[3 * C + c]) * inv_n);
    __syncthreads();
    if (threadIdx.x < R) {
        float t = 0;
        for (int k = 0; k < C; ++k) t += s[k] * __bfloat162float(wd[k * R + threadIdx.x]);
        t = rbf(t);
        h[threadIdx.x] = rbf(t / (1.f + __expf(-t)));
    }
    __syncthreads();
    if (threadIdx.x < C) {
        float t = 0;
        for (int k = 0; k < R; ++k) t += h[k] * __bfloat162float(wu[k * C + c]);
        t = rbf(t);
        g[c] = rbf(1.f / (1.f + __expf(-t)));
    }
}

__device__ __forceinline__ unsigned pk(float a, float b) {
    typedef __bf16 bf2 __attribute__((ext_vector_type(2)));
    typedef float f2 __attribute__((ext_vector_type(2)));
    f2 f = {a, b};
    bf2 r = __builtin_convertvector(f, bf2);
    return __builtin_bit_cast(unsigned, r);
}

__global__ __launch_bounds__(T1) void k_scale(const v4u* __restrict__ x, const float* __restrict__ g, v4u* __restrict__ out, int nchunks) {
    int per = nchunks / gridDim.x;
    const v4u* p = x + (size_t)blockIdx.x * per + threadIdx.x;
    v4u* o = out + (size_t)blockIdx.x * per + threadIdx.x;
    float gg[8];
    int cg = threadIdx.x % 24;
#pragma unroll
    for (int j = 0; j < 8; ++j) gg[j] = g[cg * 8 + j];
    constexpr int U = 8;
    for (int i = 0; i < per; i += T1 * U) {
        v4u v[U];
#pragma unroll
        for (int u = 0; u < U; ++u) v[u] = p[i + u * T1];
#pragma unroll
        for (int u = 0; u < U; ++u) {
            v4u r;
#pragma unroll
            for (int j = 0; j < 4; ++j) r[j] = pk(bf_lo(v[u][j]) * gg[2 * j], bf_hi(v[u][j]) * gg[2 * j + 1]);
            o[i + u * T1] = r;
        }
    }
}

extern "C" void kernel_launch(void* const* d_in, const int* in_sizes, int n_in,
                              void* d_out, int out_size, void* d_ws, size_t ws_size,
                              hipStream_t stream) {
    const v4u* x = (const v4u*)d_in[0];
    const __hip_bfloat16* wd = (const __hip_bfloat16*)d_in[1];
    const __hip_bfloat16* wu = (const __hip_bfloat16*)d_in[2];
    int n = in_sizes[0];
    int nchunks = n / 8;
    float* part = (float*)d_ws;
    float* g = part + NB1 * C;
    k_partial<<<NB1, T1, 0, stream>>>(x, part, nchunks);
    k_gate<<<1, 768, 0, stream>>>(part, wd, wu, g, 1.0f / (float)(n / C));
    k_scale<<<1024, T1, 0, stream>>>(x, g, (v4u*)d_out, nchunks);
}
```

```cpp
#include <hip/hip_runtime.h>
#include <stdint.h>

#define PACE_R 646
#define START_R 1500
#define PACE_W 610
#define ANCHOR 1040
#define WAIT1 12
#define WAIT2 127
#define WIN 10
#define AHEAD 10
#define SVC 11
#define SSTEP 3145728

typedef unsigned v4u __attribute__((ext_vector_type(4)));
typedef unsigned v2u __attribute__((ext_vector_type(2)));
typedef float v4f __attribute__((ext_vector_type(4)));
typedef float v2f __attribute__((ext_vector_type(2)));

constexpr int C = 192;
constexpr int R = 12;
constexpr int NWG = 256;
constexpr int NT = 768;
constexpr int NL = 32;
constexpr unsigned BLK = NT * 16;
constexpr unsigned XBYTES = (unsigned)NWG * NL * BLK;
constexpr int NRED = 24;
static_assert(SSTEP == NWG * BLK, "step");

constexpr unsigned WS_EPOCH = 0;
constexpr unsigned WS_S = 2048;
constexpr unsigned WS_Q = 4096;
constexpr unsigned WS_P = 8192;
constexpr unsigned WS_END = WS_P + NRED * NWG * 8 * 8;

__device__ __forceinline__ float bf_lo(unsigned u) { return __uint_as_float(u << 16); }
__device__ __forceinline__ float bf_hi(unsigned u) { return __uint_as_float(u & 0xffff0000u); }
__device__ __forceinline__ unsigned pk(float a, float b) {
    typedef __bf16 bf2 __attribute__((ext_vector_type(2)));
    v2f f = {a, b};
    bf2 r = __builtin_convertvector(f, bf2);
    return __builtin_bit_cast(unsigned, r);
}
__device__ __forceinline__ float rbf(float f) { return bf_lo(pk(f, f)); }
__device__ __forceinline__ float sigm(float z) { return __builtin_amdgcn_rcpf(1.f + __expf(-z)); }

template <int CTRL> __device__ __forceinline__ float dpp(float v) {
    return __int_as_float(__builtin_amdgcn_update_dpp(0, __float_as_int(v), CTRL, 0xf, 0xf, true));
}
__device__ __forceinline__ float add_x1(float v) { return v + dpp<0xB1>(v); }
__device__ __forceinline__ float add_x2(float v) { return v + dpp<0x4E>(v); }
__device__ __forceinline__ float add_hm(float v) { return v + dpp<0x141>(v); }
__device__ __forceinline__ float add_r4(float v) { return v + dpp<0x124>(v); }
__device__ __forceinline__ float add_r8(float v) { return v + dpp<0x128>(v); }
__device__ __forceinline__ float fold16(float a, float b) {
    auto r = __builtin_amdgcn_permlane16_swap(__float_as_uint(a), __float_as_uint(b), false, false);
    return __uint_as_float(r[0]) + __uint_as_float(r[1]);
}
__device__ __forceinline__ float fold32(float a, float b) {
    auto r = __builtin_amdgcn_permlane32_swap(__float_as_uint(a), __float_as_uint(b), false, false);
    return __uint_as_float(r[0]) + __uint_as_float(r[1]);
}
__device__ __forceinline__ unsigned rt32() { return (unsigned)__builtin_amdgcn_s_memrealtime(); }
constexpr unsigned SPIN_TICKS = 20000000u;

#define STR2(x) #x
#define STR(x) STR2(x)
#define XCLOB "v32", "v33", "v34", "v35", "v36", "v37", "v38", "v39", "v40", "v167"
#define UNPACK8 \
    "v_lshlrev_b32 v32, 16, v40\n v_and_b32 v33, s86, v40\n v_lshlrev_b32 v34, 16, v41\n v_and_b32 v35, s86, v41\n" \
    "v_lshlrev_b32 v36, 16, v42\n v_and_b32 v37, s86, v42\n v_lshlrev_b32 v38, 16, v43\n v_and_b32 v39, s86, v43\n"

__global__ __launch_bounds__(NT) __attribute__((amdgpu_num_vgpr(16)))
void se_fused(const void* __restrict__ x, void* __restrict__ out, unsigned char* __restrict__ ws,
              const unsigned short* __restrict__ wd, const unsigned short* __restrict__ wu) {
    __shared__ __attribute__((aligned(16))) float part[4 * C];
    __shared__ __attribute__((aligned(16))) float gate[C];
    __shared__ unsigned tag_s;
    float* const xw = gate;

    const unsigned t = threadIdx.x, b = blockIdx.x, lane = t & 63;
    const unsigned wave = __builtin_amdgcn_readfirstlane(t >> 6);
    asm volatile("" :: "s"(x), "s"(out), "s"(ws), "s"(wd), "s"(wu));
    const unsigned third = wave % 3, grp = wave / 3;
    const unsigned voff = (grp * 8 + (lane >> 3)) * 384 + third * 128 + (lane & 7) * 16;
    const unsigned sbase = ((b & 7) * 32 + (b >> 3)) * BLK;
    const v4u xd = {(unsigned)(uintptr_t)x, (unsigned)((uintptr_t)x >> 32) & 0xffffu, XBYTES, 0x00020000u};
    const unsigned soff = sbase + SSTEP;
    unsigned long long t64;
    asm volatile("s_nop 4\n"
                 "buffer_load_dwordx4 v[40:43], %1, %2, %3 offen nt\n"
                 "buffer_load_dwordx4 v[44:47], %1, %2, %4 offen nt\n"
                 "s_memrealtime %0\n"
                 "s_waitcnt lgkmcnt(0)\n"
                 : "=&s"(t64) : "v"(voff), "s"(xd), "s"(sbase), "s"(soff) : "memory", XCLOB);
    const unsigned tstart = (unsigned)t64;

    const auto wsr = __builtin_amdgcn_make_buffer_rsrc(ws, 0, (int)WS_END, 0x00020000);
    const auto wur = __builtin_amdgcn_make_buffer_rsrc((void*)wu, 0, C * R * 2, 0x00020000);
    const auto wdr = __builtin_amdgcn_make_buffer_rsrc((void*)wd, 0, C * R * 2, 0x00020000);
    const bool is_red = b < NRED;
    constexpr unsigned OOB = 0x80000000u;
    const unsigned wd_off = lane < 48 ? (b * 8 + (lane & 7)) * (R * 2) + (lane >> 3) * 4 : OOB;
    const unsigned due0 = (tstart << 4) + (START_R - PACE_R);
    if (wave == SVC) {
        unsigned junk;
        asm volatile("s_load_dword %0, %1, 0xc00\n s_waitcnt vmcnt(1)" : "=&s"(junk) : "s"(ws) : "memory");
        unsigned org = (rt32() << 4) - ANCHOR;
        org = (int)(org - due0) > 0 ? org : due0;
        asm volatile("" : "+s"(org), "+s"(junk));
        const unsigned ep = __builtin_amdgcn_raw_buffer_load_b32(wsr, WS_EPOCH, 0, 16);
        const unsigned pg = (b >> 3) * 2;
        const char* t0 = (pg < 24 ? (const char*)x : pg < 48 ? (const char*)out - 24 * (4u << 20) : (const char*)wu - pg * (4u << 20) - (3u << 20)) + pg * (4u << 20) + (3u << 20);
        const char* t1 = pg < 48 ? t0 + (4u << 20) : (const char*)wd;
        asm volatile("s_load_dword s88, %[t0], 0\n"
                     "s_load_dword s89, %[t1], 0\n"
                     "s_mov_b32 s90, %[so]\n"
                     ".set se_i, 2\n"
                     ".rept " STR(AHEAD) "-2\n"
                     "s_add_u32 s90, s90, " STR(SSTEP) "\n"
                     "buffer_load_dwordx4 v[40+4*se_i:43+4*se_i], %[vo], %[xd], s90 offen nt\n"
                     ".set se_i, se_i+1\n"
                     ".endr\n"
                     "s_waitcnt lgkmcnt(0)\n"
                     :: [vo] "v"(voff), [xd] "s"(xd), [so] "s"(soff), [t0] "s"(t0), [t1] "s"(t1) : "memory", "scc", "s88", "s89", "s90", XCLOB);
#pragma unroll
        for (int k = 0; k < 3; ++k)
            __builtin_amdgcn_raw_buffer_store_b64((v2u){0u, 0u}, wsr, WS_P + (((lane >> 3) * NWG + b) * 8 + (lane & 7)) * 8, k * 8 * NWG * 64, 16);
        __builtin_amdgcn_raw_buffer_store_b64((v2u){0u, 0u}, wsr, (is_red && lane < 16) ? WS_Q + (b * 16 + lane) * 8 : OOB, 0, 16);
        unsigned long long pc0; unsigned clen;
        asm volatile("s_getpc_b64 %0\n.Lpf_here_%=:\n\ts_mov_b32 %1, .Lse_code_end-.Lpf_here_%=" : "=s"(pc0), "=s"(clen));
        const auto cr = __builtin_amdgcn_make_buffer_rsrc((void*)(pc0 & ~63ull), 0, (int)clen + 64, 0x00020000);
        unsigned pf = __builtin_amdgcn_raw_buffer_load_b32(cr, b < 8 ? lane * 64 : OOB, 0, 0);
        pf ^= __builtin_amdgcn_raw_buffer_load_b32(wur, b >= NWG - 8 ? lane * 64 : OOB, 0, 0);
        pf ^= __builtin_amdgcn_raw_buffer_load_b32(wur, b >= NWG - 8 ? lane * 64 : OOB, 4096, 0);
        pf ^= __builtin_amdgcn_raw_buffer_load_b32(wdr, wd_off, 0, 0);
        unsigned tg = ep * 0x9E3779B1u + 0x7F4A7C15u;
        tg = tg ? tg : 1u;
        __builtin_amdgcn_raw_buffer_store_b64((v2u){org, tg}, wsr, (b >= NWG - 8 && lane == 0) ? WS_S + (b & 7) * 8 : OOB, 0, 16);
        tag_s = tg;
        asm volatile("" :: "v"(pf));
    }
    unsigned p_off = WS_P + (((t >> 3) * NWG + b) * 8 + (t & 7)) * 8;
    unsigned part_i = grp * C + third * 64 + (lane & 7) * 8 + (((lane >> 5) << 2) | (((lane >> 4) & 1) << 1) | ((lane >> 3) & 1));
    asm volatile("" : "+v"(part_i), "+v"(p_off));

    v2f a0, a1, a2, a3;
    v2u starts;
    unsigned my_org;
    asm volatile(
        "s_mov_b32 s94, m0\n"
        "s_getpc_b64 s[80:81]\n"
        "10:\n"
        "s_add_u32 s80, s80, 12f-10b\n s_addc_u32 s81, s81, 0\n"
        "s_add_u32 s84, s80, 15f-12f\n s_addc_u32 s85, s81, 0\n"
        "s_mov_b32 s86, 0xffff0000\n"
        "s_mov_b32 s87, 0\n"
        "s_mov_b32 s90, %[soff]\n s_mov_b32 s91, %[due]\n"
        "v_mov_b64 %[a0], 0\n v_mov_b64 %[a1], 0\n v_mov_b64 %[a2], 0\n v_mov_b64 %[a3], 0\n"
        "s_cmp_lg_u32 %[wave], " STR(SVC) "\n"
        "s_cbranch_scc1 11f\n"
        "s_waitcnt vmcnt(" STR(WIN) ")\n"
        ".rept " STR(AHEAD) "-" STR(WIN) "\n"
        "s_swappc_b64 s[82:83], s[84:85]\n"
        ".endr\n"
        "s_add_u32 s90, s90, (" STR(AHEAD) "-2)*" STR(SSTEP) "\n"
        "s_add_u32 s91, s91, (" STR(AHEAD) "-3)*" STR(PACE_R) "\n"
        "s_branch 9f\n"
        "11:\n"
        "s_waitcnt vmcnt(1)\n"
        "s_memrealtime s[88:89]\n"
        "s_waitcnt lgkmcnt(0)\n"
        "s_lshl_b32 s88, s88, 4\n"
        "s_sub_u32 s88, s88, " STR(ANCHOR) "\n"
        "s_sub_u32 s89, s88, s91\n"
        "s_cmp_gt_i32 s89, 0\n"
        "s_cselect_b32 s91, s88, s91\n"
        ".set se_i, 2\n"
        ".set se_k, 0\n"
        ".rept 30\n"
        ".if se_i == " STR(AHEAD) "\n"
        "9:\n"
        ".endif\n"
        "s_swappc_b64 s[82:83], s[80:81]\n"
        "buffer_load_dwordx4 v[40+4*se_i:43+4*se_i], %[voff], %[xd], s90 offen nt\n"
        ".rept 2\n"
        ".if (se_k <= se_i - " STR(WIN) ") || (se_k <= 2*se_i - 34)\n"
        "s_waitcnt vmcnt(se_i - se_k)\n"
        "s_swappc_b64 s[82:83], s[84:85]\n"
        ".set se_k, se_k+1\n"
        ".endif\n"
        ".endr\n"
        ".if se_i == 26\n"
        "buffer_load_dwordx2 %[st], %[stoff], %[wsr], 0 offen sc1\n"
        ".endif\n"
        ".set se_i, se_i+1\n"
        ".endr\n"
        ".rept 32-se_k\n"
        "s_waitcnt vmcnt(31 - se_k)\n"
        "s_swappc_b64 s[82:83], s[84:85]\n"
        ".set se_k, se_k+1\n"
        ".endr\n"
        "s_branch 17f\n"
        "12:\n"
        "s_add_u32 s91, s91, " STR(PACE_R) "\n"
        "s_add_u32 s90, s90, " STR(SSTEP) "\n"
        "13:\n"
        "s_memrealtime s[88:89]\n"
        "s_waitcnt lgkmcnt(0)\n"
        "s_lshl_b32 s88, s88, 4\n"
        "s_sub_u32 s88, s88, s91\n"
        "s_cmp_lt_i32 s88, 0\n"
        "s_cbranch_scc0 16f\n"
        "s_sleep 1\n"
        "s_branch 13b\n"
        "15:\n"
        "s_set_gpr_idx_on s87, gpr_idx(SRC1)\n"
        "s_add_i32 s87, s87, 4\n"
        UNPACK8
        "s_set_gpr_idx_off\n"
        "v_pk_add_f32 %[a0], %[a0], v[32:33]\n v_pk_add_f32 %[a1], %[a1], v[34:35]\n"
        "v_pk_add_f32 %[a2], %[a2], v[36:37]\n v_pk_add_f32 %[a3], %[a3], v[38:39]\n"
        "16:\n"
        "s_setpc_b64 s[82:83]\n"
        "17:\n"
        "s_mov_b32 m0, s94\n"
        "s_sub_u32 %[org], s91, 30*" STR(PACE_R) "\n"
        : [a0] "=&v"(a0), [a1] "=&v"(a1), [a2] "=&v"(a2), [a3] "=&v"(a3), [st] "=&v"(starts), [org] "=s"(my_org)
        : [voff] "v"(voff), [xd] "s"(xd), [soff] "s"(soff), [due] "s"(due0), [wsr] "s"(wsr),
          [stoff] "v"(WS_S + (lane & 7) * 8), [wave] "s"(wave)
        : "memory", "scc", "s80", "s81", "s82", "s83", "s84", "s85", "s86", "s87", "s88", "s89",
          "s90", "s91", "s94", XCLOB);
    {
        const float u0 = fold32(a0.x, a2.x), u1 = fold32(a0.y, a2.y);
        const float u2 = fold32(a1.x, a3.x), u3 = fold32(a1.y, a3.y);
        const float w0 = add_r8(fold16(u0, u2)), w1 = add_r8(fold16(u1, u3));
        part[part_i] = (lane & 8) ? w1 : w0;
    }
    __syncthreads();
    unsigned tagv;
    if (wave < 3) {
        const float p0 = part[t], p1 = part[C + t], p2 = part[2 * C + t], p3 = part[3 * C + t];
        tagv = tag_s;
        float s = p0 + p1;
        asm volatile("" : "+v"(s), "+v"(tagv));
        s += p2 + p3;
        __builtin_amdgcn_raw_buffer_store_b64((v2u){__float_as_uint(s), tagv}, wsr, p_off, 0, 16);
    } else {
        tagv = tag_s;
    }
    const unsigned tag = __builtin_amdgcn_readfirstlane(tagv);
    unsigned t_last;
    {
        int d = starts.y == tag ? (int)(starts.x - my_org) >> 4 : 0;
        d = d > 0 ? d : 0;
        d = max(d, __builtin_amdgcn_update_dpp(0, d, 0xB1, 0xf, 0xf, true));
        d = max(d, __builtin_amdgcn_update_dpp(0, d, 0x4E, 0xf, 0xf, true));
        d = max(d, __builtin_amdgcn_update_dpp(0, d, 0x141, 0xf, 0xf, true));
        d = __builtin_amdgcn_readfirstlane(d);
        t_last = rt32() + (d < 300 ? d : 300);
    }
    if (is_red) {
        unsigned q_pub = ((lane & 7) == 0 && lane < 48) ? WS_Q + b * 128 + (lane >> 3) * 16 : OOB;
        unsigned xw_i = wave * 8 + (lane & 3) * 2 + (lane >> 4);
        const unsigned wdv = __builtin_amdgcn_raw_buffer_load_b32(wdr, wave == 0 ? wd_off : OOB, 0, 0);
        asm volatile("" : "+v"(q_pub), "+v"(xw_i));
        if (wave < 8) {
            const unsigned off = WS_P + b * (NWG * 8 * 8) + t * 16;
            v4u q0, q1;
            while ((int)(rt32() - t_last) < WAIT1) __builtin_amdgcn_s_sleep(1);
            for (;;) {
                asm volatile("" ::: "memory");
                q0 = __builtin_amdgcn_raw_buffer_load_b128(wsr, off, 0, 16);
                q1 = __builtin_amdgcn_raw_buffer_load_b128(wsr, off, 8192, 16);
                const bool ok = (q0.y == tag) & (q0.w == tag) & (q1.y == tag) & (q1.w == tag);
                if (__all(ok) || rt32() - tstart > SPIN_TICKS) break;
            }
            const float e = __uint_as_float(q0.x) + __uint_as_float(q1.x);
            const float o = __uint_as_float(q0.z) + __uint_as_float(q1.z);
            float f = fold16(add_r8(add_r4(e)), add_r8(add_r4(o)));
            f = fold32(f, f);
            if ((lane & 44) == 0) xw[xw_i] = f;
        }
        __syncthreads();
        if (wave == 0) {
            float s = add_r8(xw[lane]);
            s = fold16(s, s);
            s = fold32(s, s);
            s = rbf(s * (1.0f / (XBYTES / (2 * C))));
            const float h0 = add_hm(add_x2(add_x1(s * bf_lo(wdv))));
            const float h1 = add_hm(add_x2(add_x1(s * bf_hi(wdv))));
            __builtin_amdgcn_raw_buffer_store_b128((v4u){__float_as_uint(h0), tag, __float_as_uint(h1), tag}, wsr, q_pub, 0, 16);
        }
    }
    if (wave < 3) {
        float wuf[R];
#pragma unroll
        for (int k = 0; k < R; ++k) wuf[k] = bf_lo(__builtin_amdgcn_raw_buffer_load_b16(wur, t * 2, k * C * 2, 0));
#pragma unroll
        for (int k = 0; k < R; ++k) asm volatile("" : "+v"(wuf[k]));
        const unsigned rp = lane & 7, q = lane >> 3;
        const unsigned off = WS_Q + q * 128 + (rp < 6 ? rp : 0) * 16;
        v4u q0, q1, q2;
        while ((int)(rt32() - t_last) < WAIT2) __builtin_amdgcn_s_sleep(1);
        for (;;) {
            asm volatile("" ::: "memory");
            q0 = __builtin_amdgcn_raw_buffer_load_b128(wsr, off, 0, 16);
            q1 = __builtin_amdgcn_raw_buffer_load_b128(wsr, off + 1024, 0, 16);
            q2 = __builtin_amdgcn_raw_buffer_load_b128(wsr, off + 2048, 0, 16);
            const bool ok = (q0.y == tag) & (q0.w == tag) & (q1.y == tag) & (q1.w == tag) & (q2.y == tag) & (q2.w == tag);
            if (__all(ok) || rt32() - tstart > SPIN_TICKS) break;
        }
        const float e = (__uint_as_float(q0.x) + __uint_as_float(q1.x)) + __uint_as_float(q2.x);
        const float o = (__uint_as_float(q0.z) + __uint_as_float(q1.z)) + __uint_as_float(q2.z);
        float h = fold16(add_r8(e), add_r8(o));
        h = rbf(fold32(h, h));
        h = rbf(h * sigm(h));
        float hs[R];
#pragma unroll
        for (int k = 0; k < R; ++k) hs[k] = __int_as_float(__builtin_amdgcn_readlane(__float_as_int(h), (k >> 1) + (k & 1) * 16));
        __builtin_amdgcn_sched_barrier(0);
        float z0 = hs[0] * wuf[0], z1 = hs[1] * wuf[1];
#pragma unroll
        for (int k = 2; k < R; k += 2) { z0 = fmaf(hs[k], wuf[k], z0); z1 = fmaf(hs[k + 1], wuf[k + 1], z1); }
        gate[t] = rbf(sigm(rbf(z0 + z1)));
        if (b == 0 && t == 0) __builtin_amdgcn_raw_buffer_store_b32(tag, wsr, WS_EPOCH, 0, 16);
    }
    __syncthreads();
    {
        unsigned due = rt32() << 4, off = sbase;
        const unsigned cg = third * 8 + (lane & 7);
        const v4f g0 = *(const v4f*)&gate[cg * 8], g1 = *(const v4f*)&gate[cg * 8 + 4];
        const v2f ga = {g0.x, g0.y}, gb = {g0.z, g0.w}, gc = {g1.x, g1.y}, gd = {g1.z, g1.w};
        const v4u od = {(unsigned)(uintptr_t)out, (unsigned)((uintptr_t)out >> 32) & 0xffffu, XBYTES, 0x00020000u};
        asm volatile(
            "s_mov_b32 s94, m0\n"
            "s_mov_b32 s86, 0xffff0000\n"
            "s_mov_b32 s87, 0\n"
            "20:\n"
            "s_set_gpr_idx_on s87, gpr_idx(SRC1)\n"
            "s_nop 0\n"
            UNPACK8
            "s_set_gpr_idx_off\n"
            "v_pk_mul_f32 v[32:33], %[ga], v[32:33]\n v_pk_mul_f32 v[34:35], %[gb], v[34:35]\n"
            "v_pk_mul_f32 v[36:37], %[gc], v[36:37]\n v_pk_mul_f32 v[38:39], %[gd], v[38:39]\n"
            "v_cvt_pk_bf16_f32 v32, v32, v33\n v_cvt_pk_bf16_f32 v33, v34, v35\n"
            "v_cvt_pk_bf16_f32 v34, v36, v37\n v_cvt_pk_bf16_f32 v35, v38, v39\n"
            "buffer_store_dwordx4 v[32:35], %[voff], %[od], %[off] offen sc1\n"
            "s_add_u32 s87, s87, 4\n"
            "s_cmpk_eq_u32 s87, 128\n"
            "s_cbranch_scc1 22f\n"
            "s_add_u32 %[off], %[off], " STR(SSTEP) "\n"
            "s_add_u32 %[due], %[due], " STR(PACE_W) "\n"
            "21:\n"
            "s_memrealtime s[88:89]\n"
            "s_waitcnt lgkmcnt(0)\n"
            "s_lshl_b32 s88, s88, 4\n"
            "s_sub_u32 s88, s88, %[due]\n"
            "s_cmp_lt_i32 s88, 0\n"
            "s_cbranch_scc0 20b\n"
            "s_sleep 1\n"
            "s_branch 21b\n"
            "22:\n"
            "s_mov_b32 m0, s94\n"
            ".Lse_code_end:\n"
            : [off] "+s"(off), [due] "+s"(due)
            : [voff] "v"(voff), [od] "s"(od), [ga] "v"(ga), [gb] "v"(gb), [gc] "v"(gc), [gd] "v"(gd)
            : "memory", "scc", "s86", "s87", "s88", "s89", "s94", XCLOB);
    }
}

constexpr int FB_BLOCKS = 512;
__global__ __launch_bounds__(NT) void se_sums(const v4u* __restrict__ x, float* __restrict__ part, long nchunks) {
    __shared__ float red[NT * 8];
    float acc[8] = {0.f, 0.f, 0.f, 0.f, 0.f, 0.f, 0.f, 0.f};
    for (long i = (long)blockIdx.x * NT + threadIdx.x; i < nchunks; i += (long)FB_BLOCKS * NT) {
        const v4u v = x[i];
#pragma unroll
        for (int j = 0; j < 4; ++j) { acc[2 * j] += bf_lo(v[j]); acc[2 * j + 1] += bf_hi(v[j]); }
    }
#pragma unroll
    for (int j = 0; j < 8; ++j) red[threadIdx.x * 8 + j] = acc[j];
    __syncthreads();
    if (threadIdx.x < C) {
        float s = 0.f;
        for (int k = 0; k < NT / 24; ++k) s += red[k * C + threadIdx.x];
        part[blockIdx.x * C + threadIdx.x] = s;
    }
}
__global__ __launch_bounds__(C) void se_gate(const float* __restrict__ part, const unsigned short* __restrict__ wd,
                                             const unsigned short* __restrict__ wu, float* __restrict__ g, long npix) {
    __shared__ float s[C], h[R];
    const int c = threadIdx.x;
    float a = 0.f;
    for (int k = 0; k < FB_BLOCKS; ++k) a += part[k * C + c];
    s[c] = rbf(a / (float)npix);
    __syncthreads();
    if (c < R) {
        float z = 0.f;
        for (int k = 0; k < C; ++k) z = fmaf(s[k], bf_lo(wd[k * R + c]), z);
        z = rbf(z);
        h[c] = rbf(z * sigm(z));
    }
    __syncthreads();
    float z = 0.f;
    for (int k = 0; k < R; ++k) z = fmaf(h[k], bf_lo(wu[k * C + c]), z);
    g[c] = rbf(sigm(rbf(z)));
}
__global__ __launch_bounds__(NT) void se_scale(const v4u* __restrict__ x, const float* __restrict__ g, v4u* __restrict__ out, long nchunks) {
    const float* gp = g + (threadIdx.x % 24) * 8;
    for (long i = (long)blockIdx.x * NT + threadIdx.x; i < nchunks; i += (long)FB_BLOCKS * NT) {
        const v4u v = x[i];
        v4u r;
#pragma unroll
        for (int j = 0; j < 4; ++j) r[j] = pk(bf_lo(v[j]) * gp[2 * j], bf_hi(v[j]) * gp[2 * j + 1]);
        out[i] = r;
    }
}

extern "C" void kernel_launch(void* const* d_in, const int* in_sizes, int n_in,
                              void* d_out, int out_size, void* d_ws, size_t ws_size,
                              hipStream_t stream) {
    const unsigned short* wd = (const unsigned short*)d_in[1];
    const unsigned short* wu = (const unsigned short*)d_in[2];
    int dev = 0, cus = 0;
    (void)hipGetDevice(&dev);
    (void)hipDeviceGetAttribute(&cus, hipDeviceAttributeMultiprocessorCount, dev);
    if ((unsigned)in_sizes[0] == XBYTES / 2 && cus >= NWG) {
        se_fused<<<NWG, NT, 0, stream>>>(d_in[0], d_out, (unsigned char*)d_ws, wd, wu);
    } else {
        const long nchunks = in_sizes[0] / 8;
        float* part = (float*)d_ws;
        float* g = part + FB_BLOCKS * C;
        se_sums<<<FB_BLOCKS, NT, 0, stream>>>((const v4u*)d_in[0], part, nchunks);
        se_gate<<<1, C, 0, stream>>>(part, wd, wu, g, (long)(in_sizes[0] / C));
        se_scale<<<FB_BLOCKS, NT, 0, stream>>>((const v4u*)d_in[0], g, (v4u*)d_out, nchunks);
    }
}
```
